```python
import torch

def _softmax(x, axis=-1):
    return torch.softmax(x, dim=axis)
def _gelu(x):
    return torch.nn.functional.gelu(x, approximate="tanh")
def _silu(x):
    return torch.nn.functional.silu(x)
def _sigmoid(x):
    return torch.sigmoid(x)
def _layernorm(x, axis=-1, eps=1e-5):
    m = x.mean(dim=axis, keepdim=True)
    v = x.var(dim=axis, keepdim=True, unbiased=False)
    return (x - m) / torch.sqrt(v + eps)
def _scatter_add(tgt, idx, src, axis=0):
    return tgt.clone().index_add_(axis, idx, src)
def _segment_sum(src, seg, n):
    out = torch.zeros((n,) + src.shape[1:], dtype=src.dtype)
    return out.index_add_(0, seg, src)
def _pad(x, pw):
    flat = tuple(v for pair in reversed(pw) for v in pair)
    return torch.nn.functional.pad(x, flat)

kernel_name = 'recurrent_scan__sliding_block_cached__dense_activation_gelu__decode'

def setup_inputs():

    _w = (lambda a: (lambda t: t.to(torch.bfloat16) if t.is_floating_point() else t)(torch.as_tensor(a)))
    def _A_setup():
        B, Sq, D = 1, 1, 640
        Hq, Hkv, Sk, Dh = 32, 32, 4096, 128
        assert Hq % Hkv == 0, (Hq, Hkv)
        return {
            "x": _w((lambda *s: torch.randn(*s, dtype=torch.bfloat16) * 0.05)(B, Sq, D)),
            "k_cache": _w((lambda *s: torch.randn(*s, dtype=torch.bfloat16) * 3.2)(B, Hkv, Sk, Dh)),
            "v_cache": _w((lambda *s: torch.randn(*s, dtype=torch.bfloat16) * 3.2)(B, Hkv, Sk, Dh)),
            "wq": _w((lambda *s: torch.randn(*s, dtype=torch.bfloat16) * 3.2)(D, Hq * Dh) * (D ** -0.5)),
            "wo": _w((lambda *s: torch.randn(*s, dtype=torch.bfloat16) * 3.2)(Hq * Dh, D) * ((Hq * Dh) ** -0.5)),
        }
    _A_in = _A_setup()

    def _B_setup():
        B, S, D, F = 1, 1, 640, 1728
        return {
            "x": _w((lambda *s: torch.randn(*s, dtype=torch.bfloat16) * 0.05)(B, S, D)),
            "w1": _w((lambda *s: torch.randn(*s, dtype=torch.bfloat16) * 3.2)(D, F) * (D ** -0.5)),
            "b1": _w((lambda *s: torch.randn(*s, dtype=torch.bfloat16) * 3.2)(F) * 0.01),
            "w2": _w((lambda *s: torch.randn(*s, dtype=torch.bfloat16) * 0.4)(F, D) * (F ** -0.5)),
            "b2": _w((lambda *s: torch.randn(*s, dtype=torch.bfloat16) * 0.4)(D) * 0.01),
        }
    _B_in = _B_setup()

    return {
        "A_x": _A_in["x"],
        "A_k_cache": _A_in["k_cache"],
        "A_v_cache": _A_in["v_cache"],
        "A_wq": _A_in["wq"],
        "A_wo": _A_in["wo"],
        "B_x": _B_in["x"],
        "B_w1": _B_in["w1"],
        "B_b1": _B_in["b1"],
        "B_w2": _B_in["w2"],
        "B_b2": _B_in["b2"],
    }

def reference(A_x, A_k_cache, A_v_cache, A_wq, A_wo, B_x, B_w1, B_b1, B_w2, B_b2):
    def _A_ref(x, k_cache, v_cache, wq, wo):
        B, Sq, D = x.shape
        Hq, Dh = 32, 128
        Hkv, Sk = k_cache.shape[1], k_cache.shape[2]
        W = min(4096, max(256, Sk // 4))
        rep = Hq // Hkv
        q = (lambda x, *d: x.permute(*d))((x @ wq).reshape(B, Sq, Hq, Dh), 0, 2, 1, 3)
        k = (lambda x, n, axis: torch.repeat_interleave(x, n, dim=axis))(k_cache[:, :, Sk - W:, :], rep, axis=1)
        v = (lambda x, n, axis: torch.repeat_interleave(x, n, dim=axis))(v_cache[:, :, Sk - W:, :], rep, axis=1)
        scores = torch.einsum("bhqd,bhkd->bhqk", q, k) * (Dh ** -0.5)
        p = _softmax(scores, axis=-1)
        o = torch.einsum("bhqk,bhkd->bhqd", p, v)
        return (lambda x, *d: x.permute(*d))(o, 0, 2, 1, 3).reshape(B, Sq, Hq * Dh) @ wo

    def _B_ref(x, w1, b1, w2, b2):
        h = torch.einsum("bsd,df->bsf", x, w1) + b1
        h = _gelu(h)
        return torch.einsum("bsf,fd->bsd", h, w2) + b2

    x = A_x
    xn = _layernorm(x, axis=-1)
    s = _A_ref(xn, A_k_cache, A_v_cache, A_wq, A_wo)
    g = _silu(_B_ref(xn, B_w1, B_b1, B_w2, B_b2))
    return x + s * g
```

```cpp
#include <hip/hip_runtime.h>
#include <hip/hip_bf16.h>
#include <stdint.h>

#define MIMIC 1

typedef unsigned short u16;
typedef unsigned int u32;
typedef u32 u32x4 __attribute__((ext_vector_type(4)));

constexpr int D = 640, HQ = 32, DH = 128, SK = 4096, W = 1024, F = 1728;

__device__ __forceinline__ float bf2f(u16 v) { return __uint_as_float(((u32)v) << 16); }
__device__ __forceinline__ float rbf(float f) {
  __hip_bfloat16 b = __float2bfloat16(f);
  return __bfloat162float(b);
}
__device__ __forceinline__ float rb(float f) { return MIMIC ? rbf(f) : f; }

__device__ void layernorm_to_lds(const u16* x, float* xn, float* red) {
  int t = threadIdx.x;
  float s = 0.f;
  for (int i = t; i < D; i += 256) s += bf2f(x[i]);
  for (int o = 32; o; o >>= 1) s += __shfl_xor(s, o);
  if ((t & 63) == 0) red[t >> 6] = s;
  __syncthreads();
  float mean = (red[0] + red[1] + red[2] + red[3]) / D;
  __syncthreads();
  float v = 0.f;
  for (int i = t; i < D; i += 256) { float d = bf2f(x[i]) - mean; v += d * d; }
  for (int o = 32; o; o >>= 1) v += __shfl_xor(v, o);
  if ((t & 63) == 0) red[t >> 6] = v;
  __syncthreads();
  float var = (red[0] + red[1] + red[2] + red[3]) / D;
  float m = rb(mean), vv = rb(var);
  float den = rb(sqrtf(rb(vv + 1e-5f)));
  for (int i = t; i < D; i += 256) xn[i] = rb(rb(bf2f(x[i]) - m) / den);
  __syncthreads();
}

__global__ void k1(const u16* x, const u16* wq, const u16* w1, const u16* b1, float* q, float* h) {
  __shared__ float xn[D]; __shared__ float red[4]; __shared__ float part[4][64];
  layernorm_to_lds(x, xn, red);
  int b = blockIdx.x, t = threadIdx.x, c = t & 63, kg = t >> 6;
  const u16* w; int ld, col;
  if (b < 64) { w = wq; ld = HQ * DH; col = b * 64 + c; } else { w = w1; ld = F; col = (b - 64) * 64 + c; }
  float acc = 0.f;
  for (int k = kg * 160; k < kg * 160 + 160; ++k) acc += xn[k] * bf2f(w[(size_t)k * ld + col]);
  part[kg][c] = acc;
  __syncthreads();
  if (t < 64) {
    float s = part[0][c] + part[1][c] + part[2][c] + part[3][c];
    if (b < 64) q[col] = rb(s);
    else {
      float hh = rb(rb(s) + bf2f(b1[col]));
      float g = 0.5f * hh * (1.f + tanhf(0.7978845608028654f * (hh + 0.044715f * hh * hh * hh)));
      h[col] = rb(g);
    }
  }
}

__global__ void k2(const u16* kc, const u16* vc, const float* q, float* o) {
  __shared__ float sc[W]; __shared__ float qs[DH]; __shared__ float red[4]; __shared__ float part[2][DH];
  int hd = blockIdx.x, t = threadIdx.x;
  if (t < DH) qs[t] = q[hd * DH + t];
  __syncthreads();
  const u16* kb = kc + ((size_t)hd * SK + (SK - W)) * DH;
  const u16* vb = vc + ((size_t)hd * SK + (SK - W)) * DH;
  float mx = -INFINITY;
  for (int j = t; j < W; j += 256) {
    float a = 0.f;
    for (int d = 0; d < DH; ++d) a += qs[d] * bf2f(kb[(size_t)j * DH + d]);
    a = rb(rb(a) * 0.08838834764831845f);
    sc[j] = a; mx = fmaxf(mx, a);
  }
  for (int of = 32; of; of >>= 1) mx = fmaxf(mx, __shfl_xor(mx, of));
  if ((t & 63) == 0) red[t >> 6] = mx;
  __syncthreads();
  mx = fmaxf(fmaxf(red[0], red[1]), fmaxf(red[2], red[3]));
  __syncthreads();
  float sm = 0.f;
  for (int j = t; j < W; j += 256) { float e = expf(sc[j] - mx); sc[j] = e; sm += e; }
  for (int of = 32; of; of >>= 1) sm += __shfl_xor(sm, of);
  if ((t & 63) == 0) red[t >> 6] = sm;
  __syncthreads();
  sm = red[0] + red[1] + red[2] + red[3];
  int d = t & 127, half = t >> 7;
  float a = 0.f;
  for (int j = half * 512; j < half * 512 + 512; ++j) a += (sc[j] / sm) * bf2f(vb[(size_t)j * DH + d]);
  part[half][d] = a;
  __syncthreads();
  if (t < DH) o[hd * DH + t] = rb(part[0][t] + part[1][t]);
}

__global__ void k3(const u16* x, const u16* wo, const u16* w2, const u16* b2, const float* o, const float* h, __hip_bfloat16* out) {
  __shared__ float part[2][4][64];
  int b = blockIdx.x, t = threadIdx.x, c = t & 63, kg = t >> 6, col = b * 64 + c;
  float a = 0.f;
  for (int k = kg * 1024; k < kg * 1024 + 1024; ++k) a += o[k] * bf2f(wo[(size_t)k * D + col]);
  float f = 0.f;
  for (int k = kg * 432; k < kg * 432 + 432; ++k) f += h[k] * bf2f(w2[(size_t)k * D + col]);
  part[0][kg][c] = a; part[1][kg][c] = f;
  __syncthreads();
  if (t < 64) {
    float s = rb(part[0][0][c] + part[0][1][c] + part[0][2][c] + part[0][3][c]);
    float ff = rb(rb(part[1][0][c] + part[1][1][c] + part[1][2][c] + part[1][3][c]) + bf2f(b2[col]));
    float g = rb(ff / (1.f + expf(-ff)));
    out[col] = __float2bfloat16(bf2f(x[col]) + rb(s * g));
  }
}

extern "C" void kernel_launch(void* const* d_in, const int* in_sizes, int n_in,
                              void* d_out, int out_size, void* d_ws, size_t ws_size,
                              hipStream_t stream) {
  const u16* x = (const u16*)d_in[0];
  const u16* kc = (const u16*)d_in[1];
  const u16* vc = (const u16*)d_in[2];
  const u16* wq = (const u16*)d_in[3];
  const u16* wo = (const u16*)d_in[4];
  const u16* w1 = (const u16*)d_in[6];
  const u16* b1 = (const u16*)d_in[7];
  const u16* w2 = (const u16*)d_in[8];
  const u16* b2 = (const u16*)d_in[9];
  float* q = (float*)d_ws; float* h = q + 4096; float* o = h + 2048;
  k1<<<64 + 27, 256, 0, stream>>>(x, wq, w1, b1, q, h);
  k2<<<32, 256, 0, stream>>>(kc, vc, q, o);
  k3<<<10, 256, 0, stream>>>(x, wo, w2, b2, o, h, (__hip_bfloat16*)d_out);
}
```

```cpp
#include <hip/hip_runtime.h>
#include <stdint.h>

typedef unsigned int u32;
typedef unsigned short u16;
typedef unsigned long long u64;
typedef u32 u32x4 __attribute__((ext_vector_type(4)));
typedef u32 u32x2 __attribute__((ext_vector_type(2)));
typedef float f32x2 __attribute__((ext_vector_type(2)));
typedef float f32x4 __attribute__((ext_vector_type(4)));
typedef __bf16 bf16x2 __attribute__((ext_vector_type(2)));
typedef __amdgpu_buffer_rsrc_t rsrc_t;

#define STAGE 1
#define NT_AUX 2
#define TIMELINE 0

constexpr int D = 640, HQ = 32, DH = 128, SK = 4096, WIN = 1024, FF = 1728;
constexpr int NWG = 256, NTHR = 512;
constexpr int NFG = FF / 64;
constexpr u32 OOB = 0x80000000u;
constexpr int SC1 = 16;
constexpr u32 WS_E = 0;
constexpr u32 WS_QP = 4096;
constexpr u32 WS_HP = WS_QP + NWG * 1024;
constexpr u32 WS_OP = WS_HP + NWG * 512;
constexpr u32 WS_ML = WS_OP + NWG * 1024;
constexpr u32 WS_SP = WS_ML + NWG * 16;
constexpr u32 WS_FP = WS_SP + NWG * 640;
constexpr u32 WS_END = WS_FP + NWG * 640;
constexpr u32 WS_TL = 2u << 20;
constexpr int SPIN_MAX = 1 << 18;
constexpr int LDS_BYTES = 96 * 1024;

__device__ __forceinline__ rsrc_t mk(const void* p, u32 bytes) {
  return __builtin_amdgcn_make_buffer_rsrc((void*)p, (short)0, (int)bytes, 0x00020000);
}
__device__ __forceinline__ float asf(u32 v) { return __builtin_bit_cast(float, v); }
__device__ __forceinline__ u32 asu(float v) { return __builtin_bit_cast(u32, v); }
__device__ __forceinline__ float bflo(u32 v) { return asf(v << 16); }
__device__ __forceinline__ float bfhi(u32 v) { return asf(v & 0xffff0000u); }
__device__ __forceinline__ u32 pk(float lo, float hi) {
  bf16x2 r; r.x = (__bf16)lo; r.y = (__bf16)hi; return __builtin_bit_cast(u32, r);
}
__device__ __forceinline__ float rb(float f) { return (float)(__bf16)f; }
__device__ __forceinline__ float dot2(u32 a, u32 b, float c) {
  return __builtin_amdgcn_fdot2_f32_bf16(__builtin_bit_cast(bf16x2, a), __builtin_bit_cast(bf16x2, b), c, false);
}
template <int CTRL> __device__ __forceinline__ float dpp(float x) {
  return asf(__builtin_amdgcn_mov_dpp((int)asu(x), CTRL, 0xf, 0xf, true));
}
constexpr int ROR1 = 0x121, ROR2 = 0x122, ROR4 = 0x124, ROR8 = 0x128;
__device__ __forceinline__ float row_sum(float x) {
  x += dpp<ROR8>(x); x += dpp<ROR4>(x); x += dpp<ROR2>(x); x += dpp<ROR1>(x); return x;
}
__device__ __forceinline__ float x16_sum(float x) {
  auto s = __builtin_amdgcn_permlane16_swap(asu(x), asu(x), false, false); return asf(s[0]) + asf(s[1]);
}
__device__ __forceinline__ float x32_sum(float x) {
  auto s = __builtin_amdgcn_permlane32_swap(asu(x), asu(x), false, false); return asf(s[0]) + asf(s[1]);
}
__device__ __forceinline__ float x16_max(float x) {
  auto s = __builtin_amdgcn_permlane16_swap(asu(x), asu(x), false, false); return fmaxf(asf(s[0]), asf(s[1]));
}
__device__ __forceinline__ float x32_max(float x) {
  auto s = __builtin_amdgcn_permlane32_swap(asu(x), asu(x), false, false); return fmaxf(asf(s[0]), asf(s[1]));
}
__device__ __forceinline__ float wave_sum(float x) { return x32_sum(x16_sum(row_sum(x))); }
__device__ __forceinline__ f32x2 rows_reduce(const float (&a)[8]) {
  float b[4];
#pragma unroll
  for (int k = 0; k < 4; ++k) {
    auto s = __builtin_amdgcn_permlane32_swap(asu(a[k]), asu(a[k + 4]), false, false);
    b[k] = asf(s[0]) + asf(s[1]);
  }
  f32x2 r;
#pragma unroll
  for (int k = 0; k < 2; ++k) {
    auto s = __builtin_amdgcn_permlane16_swap(asu(b[k]), asu(b[k + 2]), false, false);
    r[k] = asf(s[0]) + asf(s[1]);
  }
  return r;
}
__device__ __forceinline__ void mac2(float (&acc)[8], u32x4 a, u32x4 b, u32 x) {
#pragma unroll
  for (int k = 0; k < 4; ++k) {
    u32 lo = __builtin_amdgcn_perm(b[k], a[k], 0x05040100u);
    u32 hi = __builtin_amdgcn_perm(b[k], a[k], 0x07060302u);
    acc[2 * k] = dot2(lo, x, acc[2 * k]);
    acc[2 * k + 1] = dot2(hi, x, acc[2 * k + 1]);
  }
}
#define BAR() asm volatile("s_waitcnt lgkmcnt(0)\n\ts_barrier" ::: "memory")
#define PIN() do { asm volatile("" ::: "memory"); __builtin_amdgcn_sched_barrier(0); } while (0)
#define LD128(r, v, s, a) __builtin_amdgcn_raw_buffer_load_b128((r), (int)(v), (int)(s), (a))
#define ST128(d, r, v, s, a) __builtin_amdgcn_raw_buffer_store_b128((d), (r), (int)(v), (int)(s), (a))

#define STAMP(i) do { } while (0)

struct Smem {
  u32 xn[D / 2];
  u32 tag; u32 pad0[3];
  u32 q[DH / 2];
  float mw[8], lw[8];
  float red[8][128];
  float red2[8][128];
  float red3[8][128];
  float red4[8][128];
};

__global__ void __launch_bounds__(NTHR, 2)
fused_decode(const u16* __restrict__ x, const u16* __restrict__ kc, const u16* __restrict__ vc,
             const u16* __restrict__ wq, const u16* __restrict__ wo, const u16* __restrict__ w1,
             const u16* __restrict__ b1, const u16* __restrict__ w2, const u16* __restrict__ b2,
             u16* __restrict__ out, unsigned char* __restrict__ ws) {
  extern __shared__ __attribute__((aligned(16))) unsigned char lds_raw[];
  Smem& sm = *reinterpret_cast<Smem*>(lds_raw);
  STAMP(0);
  const int tid = threadIdx.x, lane = tid & 63;
  const int w = __builtin_amdgcn_readfirstlane(tid >> 6);
  const int r = lane >> 4, c = lane & 15;
  const int bid = blockIdx.x;
  const int h = (bid & 7) + 8 * (bid >> 6), j = (bid >> 3) & 7;
  const int lb = h * 8 + j;
  const bool ffn = h < NFG;

  const rsrc_t rws = mk(ws, WS_END);
  u32x2 xa, xb; u32 xc, ep;
  if (w == 7) {
    const rsrc_t rx = mk(x, D * 2);
    xa = __builtin_amdgcn_raw_buffer_load_b64(rx, lane * 8, 0, 0);
    xb = __builtin_amdgcn_raw_buffer_load_b64(rx, lane * 8 + 512, 0, 0);
    xc = __builtin_amdgcn_raw_buffer_load_b32(rx, lane * 4 + 1024, 0, 0);
    ep = __builtin_amdgcn_raw_buffer_load_b32(rws, WS_E, 0, SC1);
  }
  PIN();
  u32x4 p1[4];
  int xrow;
  {
    const bool isq = w < 5;
    const rsrc_t rp = isq ? mk(wq, D * HQ * DH * 2) : mk(w1, D * FF * 2);
    const u32 pitch = isq ? HQ * DH * 2 : FF * 2;
    const int rel = isq ? 16 * w + 4 * r : 32 * (w - 5) + 4 * (lane >> 3);
    xrow = 80 * j + rel;
    u32 off = (u32)xrow * pitch + (isq ? h * 256 + c * 16 : h * 128 + (lane & 7) * 16);
    if (rel >= 80 || (!isq && !ffn)) { off = OOB; xrow = 0; }
#pragma unroll
    for (int i = 0; i < 4; ++i) p1[i] = LD128(rp, off, i * pitch, NT_AUX);
  }
  PIN();
  const u32 kvoff = (u32)(((h * SK + (SK - WIN) + 128 * j + 16 * w + r) * DH + 8 * c) * 2);
  u32x4 kk[4], vv[4], wo_r[4], w2_r[2];
  {
    const rsrc_t rk = mk(kc, (u32)HQ * SK * DH * 2);
#pragma unroll
    for (int i = 0; i < 4; ++i) kk[i] = LD128(rk, kvoff, i * 1024, NT_AUX);
  }
  PIN();
  const rsrc_t rv = mk(vc, (u32)HQ * SK * DH * 2);
  const rsrc_t rwo = mk(wo, HQ * DH * D * 2);
  const rsrc_t rw2 = mk(w2, FF * D * 2);
  const u32 wooff = c < 10 ? (u32)(((h * DH + 16 * w + 4 * r) * D + 80 * j + 8 * c) * 2) : OOB;
  const u32 w2off = (c < 10 && ffn) ? (u32)(((h * 64 + 8 * w + 2 * r) * D + 80 * j + 8 * c) * 2) : OOB;
#define ISSUE_V(a, b) _Pragma("unroll") for (int i = (a); i < (b); ++i) vv[i] = LD128(rv, kvoff, i * 1024, NT_AUX)
#define ISSUE_W2() _Pragma("unroll") for (int i = 0; i < 2; ++i) w2_r[i] = LD128(rw2, w2off, i * D * 2, NT_AUX)
#define ISSUE_WO() _Pragma("unroll") for (int i = 0; i < 4; ++i) wo_r[i] = LD128(rwo, wooff, i * D * 2, NT_AUX)
  const u32 b1v = __builtin_amdgcn_raw_buffer_load_b32(mk(b1, FF * 2), ffn ? (h * 64 + 8 * w + 2 * r) * 2 : OOB, 0, 0);
  if (w == 7) {
    float v[10];
    v[0] = bflo(xa.x); v[1] = bfhi(xa.x); v[2] = bflo(xa.y); v[3] = bfhi(xa.y);
    v[4] = bflo(xb.x); v[5] = bfhi(xb.x); v[6] = bflo(xb.y); v[7] = bfhi(xb.y);
    v[8] = bflo(xc); v[9] = bfhi(xc);
    float s = 0.f;
#pragma unroll
    for (int i = 0; i < 10; ++i) s += v[i];
    const float mean = wave_sum(s) * (1.f / D);
    float q = 0.f;
#pragma unroll
    for (int i = 0; i < 10; ++i) { const float d = v[i] - mean; q = fmaf(d, d, q); }
    const float var = wave_sum(q) * (1.f / D);
    const float m = rb(mean);
    const float den = rb(sqrtf(rb(rb(var) + 1e-5f)));
#pragma unroll
    for (int i = 0; i < 10; ++i) v[i] = rb(v[i] - m) / den;
    *reinterpret_cast<u32x2*>(&sm.xn[lane * 2]) = u32x2{pk(v[0], v[1]), pk(v[2], v[3])};
    *reinterpret_cast<u32x2*>(&sm.xn[128 + lane * 2]) = u32x2{pk(v[4], v[5]), pk(v[6], v[7])};
    sm.xn[256 + lane] = pk(v[8], v[9]);
    if (lane == 0) sm.tag = ep + 0x9E3779B9u;
  }
  BAR();
  STAMP(1);
  const u32 tag = __builtin_amdgcn_readfirstlane(sm.tag);
  {
    const u32x2 xp = *reinterpret_cast<const u32x2*>(&sm.xn[xrow >> 1]);
    float acc[8] = {0.f, 0.f, 0.f, 0.f, 0.f, 0.f, 0.f, 0.f};
    mac2(acc, p1[0], p1[1], xp.x);
    mac2(acc, p1[2], p1[3], xp.y);
    *reinterpret_cast<f32x2*>(&sm.red[w][8 * c + 2 * r]) = rows_reduce(acc);
  }
  BAR();
  STAMP(2);
  if (w == 0) {
    f32x2 s = *reinterpret_cast<const f32x2*>(&sm.red[0][2 * lane]);
#pragma unroll
    for (int k = 1; k < 5; ++k) s += *reinterpret_cast<const f32x2*>(&sm.red[k][2 * lane]);
    ST128((u32x4{asu(s.x), tag, asu(s.y), tag}), rws, lane * 16, WS_QP + lb * 1024, SC1);
  } else if (w == 1) {
    const int l5 = lane & 31;
    f32x2 s = f32x2{0.f, 0.f};
#pragma unroll
    for (int k = 5; k < 8; ++k) {
      s += *reinterpret_cast<const f32x2*>(&sm.red[k][2 * l5]);
      s += *reinterpret_cast<const f32x2*>(&sm.red[k][64 + 2 * l5]);
    }
    ST128((u32x4{asu(s.x), tag, asu(s.y), tag}), rws, (lane < 32 && ffn) ? lane * 16 : OOB, WS_HP + lb * 512, SC1);
  }
  PIN();
  ISSUE_V(0, 4); PIN();
  u32 hpair;
  {
    const u32 qo = (u32)((lane >> 3) * 1024 + (16 * w + 2 * (lane & 7)) * 8);
    const u32 ho = ffn ? (u32)((c >> 1) * 512 + (8 * w + 2 * r) * 8) : OOB;
    u32x4 g, gh;
    int spins = 0;
    bool ok;
    do {
      asm volatile("" ::: "memory");
      g = LD128(rws, qo, WS_QP + h * 8192, SC1);
      gh = LD128(rws, ho, WS_HP + h * 4096, SC1);
      ok = g.y == tag && g.w == tag && (!ffn || (gh.y == tag && gh.w == tag));
    } while (!__all(ok) && ++spins < SPIN_MAX);
    STAMP(3);
    float a = asf(g.x), b = asf(g.z);
    a += dpp<ROR8>(a); b += dpp<ROR8>(b);
    auto s = __builtin_amdgcn_permlane32_swap(asu(a), asu(b), false, false);
    float t = x16_sum(asf(s[0]) + asf(s[1]));
    if ((lane & 24) == 0)
      reinterpret_cast<__bf16*>(sm.q)[16 * w + 2 * (lane & 7) + (lane >> 5)] = (__bf16)t;
    float h0 = asf(gh.x), h1 = asf(gh.z);
    h0 += dpp<ROR8>(h0); h1 += dpp<ROR8>(h1);
    h0 += dpp<ROR4>(h0); h1 += dpp<ROR4>(h1);
    h0 += dpp<ROR2>(h0); h1 += dpp<ROR2>(h1);
    h0 = rb(rb(h0) + bflo(b1v)); h1 = rb(rb(h1) + bfhi(b1v));
    const float u0 = 1.5957691216057308f * (h0 + 0.044715f * h0 * h0 * h0);
    const float u1 = 1.5957691216057308f * (h1 + 0.044715f * h1 * h1 * h1);
    hpair = pk(h0 / (1.f + __expf(-u0)), h1 / (1.f + __expf(-u1)));
  }
  PIN(); ISSUE_W2(); ISSUE_WO(); PIN();
  BAR();
  {
    const u32x4 qq = *reinterpret_cast<const u32x4*>(&sm.q[4 * c]);
    float s[4];
#pragma unroll
    for (int i = 0; i < 4; ++i) {
      float d = dot2(kk[i][0], qq[0], 0.f);
      d = dot2(kk[i][1], qq[1], d); d = dot2(kk[i][2], qq[2], d); d = dot2(kk[i][3], qq[3], d);
      s[i] = rb(rb(row_sum(d)) * 0.08838834764831845f);
    }
    const float m = x32_max(x16_max(fmaxf(fmaxf(s[0], s[1]), fmaxf(s[2], s[3]))));
    float p[4];
#pragma unroll
    for (int i = 0; i < 4; ++i) p[i] = __expf(s[i] - m);
    const float l = x32_sum(x16_sum((p[0] + p[1]) + (p[2] + p[3])));
    STAMP(4);
    float acc[8] = {0.f, 0.f, 0.f, 0.f, 0.f, 0.f, 0.f, 0.f};
#pragma unroll
    for (int i = 0; i < 4; ++i)
#pragma unroll
      for (int k = 0; k < 4; ++k) {
        acc[2 * k] = fmaf(p[i], bflo(vv[i][k]), acc[2 * k]);
        acc[2 * k + 1] = fmaf(p[i], bfhi(vv[i][k]), acc[2 * k + 1]);
      }
    *reinterpret_cast<f32x2*>(&sm.red2[w][8 * c + 2 * r]) = rows_reduce(acc);
    if (lane == 0) { sm.mw[w] = m; sm.lw[w] = l; }
  }
  BAR();
  STAMP(5);
  if (w == 0) {
    const f32x4 m0 = *reinterpret_cast<const f32x4*>(&sm.mw[0]), m1 = *reinterpret_cast<const f32x4*>(&sm.mw[4]);
    const f32x4 l0 = *reinterpret_cast<const f32x4*>(&sm.lw[0]), l1 = *reinterpret_cast<const f32x4*>(&sm.lw[4]);
    const float M = fmaxf(fmaxf(fmaxf(m0.x, m0.y), fmaxf(m0.z, m0.w)), fmaxf(fmaxf(m1.x, m1.y), fmaxf(m1.z, m1.w)));
    f32x2 o = f32x2{0.f, 0.f};
    float L = 0.f;
#pragma unroll
    for (int k = 0; k < 8; ++k) {
      const float e = __expf((k < 4 ? m0[k & 3] : m1[k & 3]) - M);
      L = fmaf(e, k < 4 ? l0[k & 3] : l1[k & 3], L);
      o += e * *reinterpret_cast<const f32x2*>(&sm.red2[k][2 * lane]);
    }
    ST128((u32x4{asu(o.x), tag, asu(o.y), tag}), rws, lane * 16, WS_OP + lb * 1024, SC1);
    ST128((u32x4{asu(M), tag, asu(L), tag}), rws, lane == 0 ? 0 : OOB, WS_ML + lb * 16, SC1);
  }
  PIN();
  {
    float acc[8] = {0.f, 0.f, 0.f, 0.f, 0.f, 0.f, 0.f, 0.f};
    mac2(acc, w2_r[0], w2_r[1], hpair);
    *reinterpret_cast<f32x2*>(&sm.red4[w][8 * c + 2 * r]) = rows_reduce(acc);
  }
  PIN();
  {
    const u32 oo = (u32)((c >> 1) * 1024 + (16 * w + 4 * r + 2 * (c & 1)) * 8);
    const u32 mo = (u32)((c >> 1) * 16);
    u32x4 g, gm;
    int spins = 0;
    bool ok;
    do {
      asm volatile("" ::: "memory");
      g = LD128(rws, oo, WS_OP + h * 8192, SC1);
      gm = LD128(rws, mo, WS_ML + h * 128, SC1);
      ok = g.y == tag && g.w == tag && gm.y == tag && gm.w == tag;
    } while (!__all(ok) && ++spins < SPIN_MAX);
    STAMP(6);
    const float mp = asf(gm.x);
    float M = fmaxf(mp, dpp<ROR8>(mp)); M = fmaxf(M, dpp<ROR4>(M)); M = fmaxf(M, dpp<ROR2>(M));
    const float e = __expf(mp - M);
    float L = e * asf(gm.z), a = e * asf(g.x), b = e * asf(g.z);
    L += dpp<ROR8>(L); a += dpp<ROR8>(a); b += dpp<ROR8>(b);
    L += dpp<ROR4>(L); a += dpp<ROR4>(a); b += dpp<ROR4>(b);
    L += dpp<ROR2>(L); a += dpp<ROR2>(a); b += dpp<ROR2>(b);
    const float inv = 1.f / L;
    const u32 op = pk(a * inv, b * inv);
    const u32 o01 = (u32)__builtin_amdgcn_mov_dpp((int)op, 0xA0, 0xf, 0xf, true);
    const u32 o23 = (u32)__builtin_amdgcn_mov_dpp((int)op, 0xF5, 0xf, 0xf, true);
    float acc[8] = {0.f, 0.f, 0.f, 0.f, 0.f, 0.f, 0.f, 0.f};
    mac2(acc, wo_r[0], wo_r[1], o01);
    mac2(acc, wo_r[2], wo_r[3], o23);
    *reinterpret_cast<f32x2*>(&sm.red3[w][8 * c + 2 * r]) = rows_reduce(acc);
  }
  BAR();
  STAMP(7);
  if (w < 2) {
    const float (*rp)[128] = w == 0 ? sm.red3 : sm.red4;
    f32x2 s = *reinterpret_cast<const f32x2*>(&rp[0][2 * lane]);
#pragma unroll
    for (int k = 1; k < 8; ++k) s += *reinterpret_cast<const f32x2*>(&rp[k][2 * lane]);
    const bool on = lane < 40 && (w == 0 || ffn);
    ST128((u32x4{asu(s.x), tag, asu(s.y), tag}), rws, on ? lane * 16 : OOB, (w == 0 ? WS_SP : WS_FP) + lb * 640, SC1);
  } else if (w == 2 && !ffn) {
    const int cg = (h - NFG) * 8 + j, jc = cg / 5, sub = cg % 5;
    const int pp = lane >> 3, k = lane & 7;
    const int col = 80 * jc + 16 * sub + 2 * k;
    const u32 xv = __builtin_amdgcn_raw_buffer_load_b32(mk(x, D * 2), col * 2, 0, 0);
    const u32 bv = __builtin_amdgcn_raw_buffer_load_b32(mk(b2, D * 2), col * 2, 0, 0);
    const u32 go = (u32)(pp * 8 * 640 + jc * 640 + sub * 128 + k * 16);
    const u32 go3 = pp < 3 ? go : OOB;
    u32x4 g[8];
    int spins = 0;
    bool ok;
    do {
      asm volatile("" ::: "memory");
#pragma unroll
      for (int n = 0; n < 4; ++n) g[n] = LD128(rws, go, WS_SP + n * 64 * 640, SC1);
#pragma unroll
      for (int n = 0; n < 4; ++n) g[4 + n] = LD128(rws, n < 3 ? go : go3, WS_FP + n * 64 * 640, SC1);
      ok = true;
#pragma unroll
      for (int n = 0; n < 7; ++n) ok = ok && g[n].y == tag && g[n].w == tag;
      ok = ok && (pp >= 3 || (g[7].y == tag && g[7].w == tag));
    } while (!__all(ok) && ++spins < SPIN_MAX);
    STAMP(8);
    float s0 = (asf(g[0].x) + asf(g[1].x)) + (asf(g[2].x) + asf(g[3].x));
    float s1 = (asf(g[0].z) + asf(g[1].z)) + (asf(g[2].z) + asf(g[3].z));
    float f0 = (asf(g[4].x) + asf(g[5].x)) + (asf(g[6].x) + asf(g[7].x));
    float f1 = (asf(g[4].z) + asf(g[5].z)) + (asf(g[6].z) + asf(g[7].z));
    s0 += dpp<ROR8>(s0); s1 += dpp<ROR8>(s1); f0 += dpp<ROR8>(f0); f1 += dpp<ROR8>(f1);
    s0 = x32_sum(x16_sum(s0)); s1 = x32_sum(x16_sum(s1)); f0 = x32_sum(x16_sum(f0)); f1 = x32_sum(x16_sum(f1));
    f0 = rb(rb(f0) + bflo(bv)); f1 = rb(rb(f1) + bfhi(bv));
    const float g0 = rb(f0 / (1.f + __expf(-f0))), g1 = rb(f1 / (1.f + __expf(-f1)));
    const u32 res = pk(bflo(xv) + rb(rb(s0) * g0), bfhi(xv) + rb(rb(s1) * g1));
    __builtin_amdgcn_raw_buffer_store_b32(res, mk(out, D * 2), lane < 8 ? col * 2 : OOB, 0, 0);
    if (cg == 0) __builtin_amdgcn_raw_buffer_store_b32(tag, rws, lane == 0 ? WS_E : OOB, 0, SC1);
  }
}

extern "C" void kernel_launch(void* const* d_in, const int* in_sizes, int n_in, void* d_out, int out_size,
                              void* d_ws, size_t ws_size, hipStream_t stream) {
  (void)hipFuncSetAttribute((const void*)fused_decode, hipFuncAttributeMaxDynamicSharedMemorySize, LDS_BYTES);
  fused_decode<<<NWG, NTHR, LDS_BYTES, stream>>>(
      (const u16*)d_in[0], (const u16*)d_in[1], (const u16*)d_in[2], (const u16*)d_in[3], (const u16*)d_in[4],
      (const u16*)d_in[6], (const u16*)d_in[7], (const u16*)d_in[8], (const u16*)d_in[9], (u16*)d_out,
      (unsigned char*)d_ws);
}
```

```cpp
#define CODE_BYTES 6850
#define WND_T 20
#include <hip/hip_runtime.h>
#include <stdint.h>

typedef unsigned int u32;
typedef unsigned short u16;
typedef unsigned long long u64;
typedef u32 u32x4 __attribute__((ext_vector_type(4)));
typedef u32 u32x2 __attribute__((ext_vector_type(2)));
typedef u32 u32x8 __attribute__((ext_vector_type(8)));
typedef float f32x2 __attribute__((ext_vector_type(2)));
typedef float f32x4 __attribute__((ext_vector_type(4)));
typedef short s16x4 __attribute__((ext_vector_type(4)));
typedef __bf16 bf16x2 __attribute__((ext_vector_type(2)));
typedef __bf16 bf16x8 __attribute__((ext_vector_type(8)));
typedef __amdgpu_buffer_rsrc_t rsrc_t;

#define TIMELINE 0
#define DIAG_WAVE 0
#define WS_AT_END 0
#define STR2(x) #x
#define STR(x) STR2(x)
#define SLEEPS(n) "s_sleep " STR(n) "\n\t"
#define WND 24
#define WND_V WND
#define OTRIG 118
#define ABL 0
#define SPIN2 0
#define SL_O 0
#define SL_R 0
#define SL_M 0
#define PWFIRST 0
#define PRE_EARLY 0
#define TRFIRST 1
#define PRE_SF 0
#define QTRIG 44
#define OGAP 10
#define MTRIG 16
#define SPIN_SL 1

constexpr int D = 640, HQ = 32, DH = 128, SK = 4096, WIN = 1024, FF = 1728;
constexpr int NWG = 256, NTHR = 640;
constexpr int NFG = FF / 64;
constexpr u32 OOB = 0x80000000u;
constexpr int SC1 = 16;
constexpr u32 WS_E = 0;
constexpr u32 WS_QP = 4096;
constexpr u32 WS_HP = WS_QP + NWG * 1024;
constexpr u32 WS_OP = WS_HP + NWG * 512;
constexpr u32 WS_ML = WS_OP + NWG * 1024;
constexpr u32 WS_SP = WS_ML + NWG * 128;
constexpr u32 WS_FP = WS_SP + HQ * D * 8;
constexpr u32 WS_END = WS_FP + NFG * D * 8;
constexpr u32 WS_TL = 2u << 20;
constexpr int SPIN_MAX = 1 << 18;
constexpr u32 S_CTL = 0;
constexpr u32 S_ZERO = 0;
constexpr u32 S_FX = 16, S_FQ = 20;
constexpr u32 S_MW = 32, S_LW = 64, S_PW = 96;
constexpr u32 S_TAG = 128;
constexpr u32 S_PROG = 256;
constexpr u32 S_XN = 512;
constexpr u32 S_Q = 1792;
constexpr u32 S_P = 2048;
constexpr u32 S_AV = 2304;
constexpr u32 S_RED = 2816;
constexpr u32 S_WQ = 4096;
constexpr int N_WQ = 20, N_W1 = 30, N_K = 62, N_V = 94, N_B = 110, N_A = 126;
constexpr u32 S_W1 = S_WQ + N_WQ * 1024, S_K = S_WQ + N_W1 * 1024, S_V = S_WQ + N_K * 1024, S_B = S_WQ + N_V * 1024,
              S_A = S_WQ + N_B * 1024;
constexpr int LDS_BYTES = S_WQ + N_A * 1024;
constexpr u32 UNSET = 0xbf800001u;
#define LDS(T, off) (*(__attribute__((address_space(3))) T*)(off))

__device__ __forceinline__ rsrc_t mk(const void* p, u32 bytes) {
  return __builtin_amdgcn_make_buffer_rsrc((void*)p, (short)0, (int)bytes, 0x00020000);
}
__device__ __forceinline__ float asf(u32 v) { return __builtin_bit_cast(float, v); }
__device__ __forceinline__ u32 asu(float v) { return __builtin_bit_cast(u32, v); }
__device__ __forceinline__ float bflo(u32 v) { return asf(v << 16); }
__device__ __forceinline__ float bfhi(u32 v) { return asf(v & 0xffff0000u); }
__device__ __forceinline__ u32 pk(float lo, float hi) {
  bf16x2 r; r.x = (__bf16)lo; r.y = (__bf16)hi; return __builtin_bit_cast(u32, r);
}
__device__ __forceinline__ float rb(float f) { return (float)(__bf16)f; }
__device__ __forceinline__ float rcp(float f) { return __builtin_amdgcn_rcpf(f); }
__device__ __forceinline__ float ex2(float f) { return __builtin_amdgcn_exp2f(f); }
constexpr float LOG2E = 1.4426950408889634f;
__device__ __forceinline__ u32 duplo(u32 v) { return __builtin_amdgcn_perm(v, v, 0x01000100u); }
__device__ __forceinline__ u32 duphi(u32 v) { return __builtin_amdgcn_perm(v, v, 0x03020302u); }
__device__ __forceinline__ float add(float a, float b) {
  float r; asm("v_add_f32 %0, %1, %2" : "=v"(r) : "v"(a), "v"(b)); return r;
}
__device__ __forceinline__ float mx(float a, float b) {
  float r; asm("v_max_f32 %0, %1, %2" : "=v"(r) : "v"(a), "v"(b)); return r;
}
__device__ __forceinline__ float mx3(float a, float b, float c) {
  float r; asm("v_max3_f32 %0, %1, %2, %3" : "=v"(r) : "v"(a), "v"(b), "v"(c)); return r;
}
#define MXDPP(x, ctrl) asm("s_nop 1\n\tv_max_f32_dpp %0, %0, %0 " ctrl " row_mask:0xf bank_mask:0xf" : "+v"(x))
template <int CTRL> __device__ __forceinline__ u32 dppu(u32 x) {
  return (u32)__builtin_amdgcn_mov_dpp((int)x, CTRL, 0xf, 0xf, true);
}
template <int CTRL> __device__ __forceinline__ float dpp(float x) { return asf(dppu<CTRL>(asu(x))); }
constexpr int ROR1 = 0x121, ROR2 = 0x122, ROR4 = 0x124, ROR8 = 0x128, QX1 = 0xB1, QX2 = 0x4E, BC = 0x150;
__device__ __forceinline__ float x16_sum(float x) {
  auto s = __builtin_amdgcn_permlane16_swap(asu(x), asu(x), false, false); return asf(s[0]) + asf(s[1]);
}
__device__ __forceinline__ float x32_sum(float x) {
  auto s = __builtin_amdgcn_permlane32_swap(asu(x), asu(x), false, false); return asf(s[0]) + asf(s[1]);
}
__device__ __forceinline__ float x16_max(float x) {
  auto s = __builtin_amdgcn_permlane16_swap(asu(x), asu(x), false, false); return mx(asf(s[0]), asf(s[1]));
}
__device__ __forceinline__ float x32_max(float x) {
  auto s = __builtin_amdgcn_permlane32_swap(asu(x), asu(x), false, false); return mx(asf(s[0]), asf(s[1]));
}
__device__ __forceinline__ float wave_sum(float x) {
  x += dpp<ROR8>(x); x += dpp<ROR4>(x); x += dpp<ROR2>(x); x += dpp<ROR1>(x);
  return x32_sum(x16_sum(x));
}
__device__ __forceinline__ f32x4 mfma(u32x4 a, u32x4 b, f32x4 c) {
  return __builtin_amdgcn_mfma_f32_16x16x32_bf16(__builtin_bit_cast(bf16x8, a), __builtin_bit_cast(bf16x8, b), c, 0, 0, 0);
}
__device__ __forceinline__ u32 bad2(u32x4 g, u32 tag) { return __builtin_amdgcn_bitop3_b32(g.y, g.w, tag, 0x7e); }
__device__ __forceinline__ u32 bad4(const u32x4* g, u32 tag) {
  return (bad2(g[0], tag) | bad2(g[1], tag)) | (bad2(g[2], tag) | bad2(g[3], tag));
}
__device__ __forceinline__ bool none(u32 bad) {
  asm volatile("" : "+v"(bad));
  return __builtin_amdgcn_ballot_w64(bad != 0) == 0;
}
__device__ __forceinline__ float sx4(const u32x4* g) { return add(add(asf(g[0].x), asf(g[1].x)), add(asf(g[2].x), asf(g[3].x))); }
__device__ __forceinline__ float sz4(const u32x4* g) { return add(add(asf(g[0].z), asf(g[1].z)), add(asf(g[2].z), asf(g[3].z))); }

#define PIN() do { asm volatile("" ::: "memory"); __builtin_amdgcn_sched_barrier(0); } while (0)
#define BAR() do { asm volatile("s_waitcnt lgkmcnt(0)\n\ts_barrier" ::: "memory"); __builtin_amdgcn_sched_barrier(0); } while (0)
#define RBAR() do { PIN(); asm volatile("s_barrier" ::: "memory"); PIN(); } while (0)
#define LD128(r, v, s, a) __builtin_amdgcn_raw_buffer_load_b128((r), (int)(v), (int)(s), (a))
#define LD32(r, v, s, a) __builtin_amdgcn_raw_buffer_load_b32((r), (int)(v), (int)(s), (a))
#define ST128(d, r, v, s, a) __builtin_amdgcn_raw_buffer_store_b128((d), (r), (int)(v), (int)(s), (a))
#define PUB(a, b, v, s) ST128((u32x4{asu(a), tag, asu(b), tag}), rws, (v), (s), SC1)
#define OPAQUE(v) int v = wv; asm volatile("" : "+s"(v))
#define Z4 (f32x4{0.f, 0.f, 0.f, 0.f})
#define ANCH4(g) asm volatile("" : "+v"((g)[0]), "+v"((g)[1]), "+v"((g)[2]), "+v"((g)[3]))
#define TR_READ(off) __builtin_bit_cast(u32x2, __builtin_amdgcn_ds_read_tr16_b64_v4i16( \
    (__attribute__((address_space(3))) s16x4*)(off)))
#define POLL(g, A, B, C, voff, tagv, SLP) do { u32 t_; int n_; \
    asm volatile("s_mov_b32 %2, 0x200000\n\ts_waitcnt vmcnt(0)\n\t" \
                 "v_bitop3_b32 %1, v" #B ", v" #C ", %5 bitop3:0x7e\n\t" \
                 "v_cmp_ne_u32 vcc, 0, %1\n\t" \
                 "s_cbranch_vccz 2f\n" \
                 "1:\n\t" SLP "buffer_load_dwordx4 v[" #A ":" #C "], %3, %4, 0 offen sc1\n\t" \
                 "s_sub_u32 %2, %2, 1\n\ts_waitcnt vmcnt(0)\n\t" \
                 "v_bitop3_b32 %1, v" #B ", v" #C ", %5 bitop3:0x7e\n\t" \
                 "v_cmp_ne_u32 vcc, 0, %1\n\t" \
                 "s_cbranch_scc1 2f\n\ts_cbranch_vccnz 1b\n2:" \
                 : "+{v[" #A ":" #C "]}"(g), "=&v"(t_), "=&s"(n_) : "v"(voff), "s"(rws), "v"(tagv) : "vcc", "scc", "memory"); } while (0)

#define REFRESH(g, A, B, C, voff, tagv) do { u32 t_; \
    asm volatile("v_bitop3_b32 %1, v" #B ", v" #C ", %4 bitop3:0x7e\n\t" \
                 "v_cmp_ne_u32 vcc, 0, %1\n\t" \
                 "s_cbranch_vccz 2f\n\t" \
                 "buffer_load_dwordx4 v[" #A ":" #C "], %2, %3, 0 offen sc1\n2:" \
                 : "+{v[" #A ":" #C "]}"(g), "=&v"(t_) : "v"(voff), "s"(rws), "v"(tagv) : "vcc", "memory"); } while (0)

#define WAIT_GE(addr, thr, SLP) do { u32 t_; \
    asm volatile("1:\n\tds_read_b32 %0, %1\n\ts_waitcnt lgkmcnt(0)\n\tv_cmp_gt_i32 vcc, %2, %0\n\t" \
                 "s_cbranch_vccz 2f\n\t" SLP "s_branch 1b\n2:" : "=&v"(t_) : "v"(addr), "s"(thr) : "vcc", "memory"); } while (0)
#define WAIT_SET(val, addr, SLP) \
    asm volatile("1:\n\tds_read_b32 %0, %1\n\ts_waitcnt lgkmcnt(0)\n\tv_cmp_eq_u32 vcc, %2, %0\n\t" \
                 "s_cbranch_vccz 2f\n\t" SLP "s_branch 1b\n2:" : "=&v"(val) : "v"(addr), "s"(UNSET) : "vcc", "memory")
__device__ __forceinline__ u32x4 cat(u32x2 a, u32x2 b) { return u32x4{a.x, a.y, b.x, b.y}; }

#define STAMP(i) do { } while (0)

extern "C" __attribute__((aligned(4096))) __global__ void __launch_bounds__(NTHR, 3)
fused_decode(const u16* x_, unsigned char* ws_, const u16* wq_, const u16* kc_, const u16* w1_, const u16* vc_,
             const u16* wo_, const u16* w2_, const u16* b1_, const u16* b2_, u16* out_) {
#if defined(__HIP_DEVICE_COMPILE__)
  const int tid = threadIdx.x, lane = tid & 63;
  u32 l4 = lane * 4;
  {
    u32 ia, iv;
    asm volatile("v_cmp_lt_u32 vcc, 0x1ff, %2\n\ts_cbranch_vccz 1f\n\t"
                 "v_subrev_u32 %0, 0x208, %2\n\tv_cmp_gt_u32 vcc, 24, %0\n\tv_mov_b32 %1, %3\n\tv_lshlrev_b32 %0, 2, %2\n\t"
                 "v_cndmask_b32 %1, 0, %1, vcc\n\tv_and_b32 %0, 0x1ff, %0\n\t"
                 "ds_write_b32 %0, %1\n\ts_waitcnt lgkmcnt(0)\n1:"
                 : "=&v"(ia), "=&v"(iv) : "v"(tid), "i"(UNSET) : "vcc", "memory");
  }
  u32x8 ka, kb; u32x4 kc4; u32x2 kd;
  asm volatile("s_load_dwordx8 %0, %4, 0x0\n\ts_load_dwordx8 %1, %4, 0x20\n\ts_load_dwordx4 %2, %4, 0x40\n\ts_load_dwordx2 %3, %4, 0x50"
               : "=&s"(ka), "=&s"(kb), "=&s"(kc4), "=&s"(kd) : "s"(__builtin_amdgcn_kernarg_segment_ptr()));
  u32 cpf;
  {
    u64 pc, sv;
    asm volatile("s_getpc_b64 %1\n\tv_cmp_gt_u32 vcc, %4, %3\n\tv_lshlrev_b32 %0, 7, %3\n\ts_and_saveexec_b64 %2, vcc\n\ts_cbranch_execz 1f\n\t"
                 "global_load_dword %0, %0, %1 offset:1024\n1:\n\ts_mov_b64 exec, %2"
                 : "=&v"(cpf), "=&s"(pc), "=&s"(sv) : "v"(tid), "i"((CODE_BYTES - 1024 - 256) / 128) : "vcc", "scc");
  }
  PIN();
  const int wv = __builtin_amdgcn_readfirstlane(tid >> 6);
  const int w = wv - 2;
  const int r = lane >> 4, c = lane & 15, g8 = lane >> 3;
  const int bid = blockIdx.x;
  const int h = (bid & 7) + 8 * (bid >> 6), j = (bid >> 3) & 7;
  const int lb = h * 8 + j;
  const bool ffn = h < NFG;
  const bool b_w2 = ffn && j < 5;
  const bool b_wo = j == 5 || j == 6;
  const int la = j < 5 ? j : 2 * j - 5;
  const u32 cw = (u32)(c ^ (r << 1)) * 16;
  u32 qoff = (u32)(80 * j + r) * (HQ * DH * 2) + h * 256 + cw;
  u32 l8 = lane * 8, vz = 0;
  u32 kb1 = 1024;
  asm volatile("" : "+v"(qoff), "+v"(l8), "+v"(l4), "+v"(vz), "+s"(kb1));
  RBAR();
  asm volatile("s_waitcnt lgkmcnt(0)" : "+s"(ka), "+s"(kb), "+s"(kc4), "+s"(kd) :: "memory");
#define PTR(lo, hi) ((unsigned char*)(((u64)(hi) << 32) | (lo)))
  unsigned char *const x = PTR(ka[0], ka[1]), *const ws = PTR(ka[2], ka[3]), *const wq = PTR(ka[4], ka[5]), *const kc = PTR(ka[6], ka[7]),
                *const w1 = PTR(kb[0], kb[1]), *const vc = PTR(kb[2], kb[3]), *const wo = PTR(kb[4], kb[5]), *const w2 = PTR(kb[6], kb[7]),
                *const b1 = PTR(kc4[0], kc4[1]), *const b2 = PTR(kc4[2], kc4[3]), *const out = PTR(kd[0], kd[1]);
  STAMP(20);
  u32x2 xa, xb; u32 xc, ep;
  asm volatile("s_cmp_lg_u32 %8, 0\n\ts_cbranch_scc1 1f\n\t"
               "global_load_dwordx2 %0, %4, %6\n\tglobal_load_dwordx2 %1, %4, %6 offset:512\n\t"
               "global_load_dword %2, %5, %6 offset:1024\n\tglobal_load_dword %3, %9, %7 sc1\n1:"
               : "=&v"(xa), "=&v"(xb), "=&v"(xc), "=&v"(ep)
               : "v"(l8), "v"(l4), "s"(x), "s"(ws), "s"(wv), "v"(vz) : "scc", "memory");
  PIN();
  if (wv == 1) {
    __builtin_amdgcn_s_setprio(3);
    u32 so;
    asm volatile("s_mov_b32 m0, %4\n\ts_mov_b32 %0, 0\n"
                 "1:\n\t"
                 ".rept 4\n\tbuffer_load_dwordx4 %1, %2, %0 offen nt lds\n\ts_add_u32 m0, m0, %6\n\ts_add_u32 %0, %0, %3\n\t.endr\n\t"
                 "s_cmp_lg_u32 m0, %5\n\ts_cbranch_scc1 1b"
                 : "=&s"(so) : "v"(qoff), "s"(mk(wq, D * HQ * DH * 2)), "s"(4 * HQ * DH * 2), "i"(S_WQ), "i"(S_W1), "s"(kb1) : "scc", "memory");
    u32 pa = S_PROG + l4, pv = (u32)(N_WQ + 2 - WND);
#define SEG(rs, voff, start, step, end, wnd) \
    asm volatile("s_mov_b32 %0, %5\n" \
                 "1:\n\ts_waitcnt vmcnt(%8)\n\t" \
                 "buffer_load_dwordx4 %2, %3, %0 offen nt lds\n\t" \
                 "s_add_u32 m0, m0, %9\n\ts_add_u32 %0, %0, %6\n\t" \
                 "buffer_load_dwordx4 %2, %3, %0 offen nt lds\n\t" \
                 "ds_write_b32 %4, %1\n\t" \
                 "s_add_u32 m0, m0, %9\n\ts_add_u32 %0, %0, %6\n\tv_add_u32 %1, 2, %1\n\t" \
                 "s_cmp_lg_u32 m0, %7\n\ts_cbranch_scc1 1b" \
                 : "=&s"(so), "+v"(pv) : "v"(voff), "s"(rs), "v"(pa), "s"(start), "s"(step), "i"(end), "i"((wnd) - 2), "s"(kb1) : "scc", "memory")
    const u32 cn = (u32)((lane & 7) ^ (((g8 >> 1) & 3) << 1)) * 16;
    const u32 foff = ffn ? (u32)(80 * j + g8) * (FF * 2) + h * 128 + cn : OOB;
    SEG(mk(w1, D * FF * 2), foff, 0, 8 * FF * 2, S_K, WND);
    const u32 kvbase = (u32)((h * SK + (SK - WIN)) * DH * 2 + j * 1024);
    const u32 koff = kvbase + ((lane >> 2) & 3) * 256 + (4 * r + (lane & 3)) * 16;
    SEG(mk(kc, (u32)HQ * SK * DH * 2), koff, 0, 8192, S_V, WND);
    const u32 voff = kvbase + r * 256 + cw;
    pv += WND - WND_V;
    SEG(mk(vc, (u32)HQ * SK * DH * 2), voff, 0, 8192, S_B, WND_V);
    const rsrc_t rwo = mk(wo, HQ * DH * D * 2);
    const rsrc_t rB = j < 5 ? mk(w2, FF * D * 2) : rwo;
    const u32 aoff = (u32)(h * DH + g8) * (D * 2) + la * 128 + cn;
    const u32 boff = b_w2 ? (u32)(h * 64 + g8) * (D * 2) + j * 256 + cn : b_wo ? aoff + 128 : OOB;
    pv += WND_V - WND_T;
    SEG(rB, boff, 0, 8 * D * 2, S_B + 8192, WND_T);
    SEG(rB, boff, (j < 5 ? 128 : 64 * D * 2), 8 * D * 2, S_A, WND_T);
    SEG(rwo, aoff, 0, 8 * D * 2, LDS_BYTES, WND_T);
    if (WND_T > 18) {
      asm volatile("s_waitcnt vmcnt(16)" ::: "memory");
      LDS(u32, pa) = N_A - 16;
    }
    asm volatile("s_waitcnt vmcnt(8)" ::: "memory");
    LDS(u32, pa) = N_A - 8;
    asm volatile("s_waitcnt vmcnt(0)" ::: "memory");
    LDS(u32, pa) = N_A;
    STAMP(19);
    asm volatile("s_waitcnt lgkmcnt(0)" ::: "memory");
    asm volatile("s_endpgm" ::: "memory");
  }
  PIN();
  if (ABL == 1 && wv != 2) { asm volatile("s_waitcnt vmcnt(0)\n\ts_endpgm" ::: "memory"); }
  const rsrc_t rws = mk(ws, WS_END);
  u32 pa = S_PROG + l4;
  u32 atag;
  OPAQUE(wv2);
  if (wv2 == 0) {
    __builtin_amdgcn_s_setprio(2);
    asm volatile("s_waitcnt vmcnt(1)" : "+v"(xa), "+v"(xb), "+v"(xc) :: "memory");
    STAMP(21);
    {
      float v[10];
      v[0] = bflo(xa.x); v[1] = bfhi(xa.x); v[2] = bflo(xa.y); v[3] = bfhi(xa.y);
      v[4] = bflo(xb.x); v[5] = bfhi(xb.x); v[6] = bflo(xb.y); v[7] = bfhi(xb.y);
      v[8] = bflo(xc); v[9] = bfhi(xc);
      const float s = (((v[0] + v[1]) + (v[2] + v[3])) + ((v[4] + v[5]) + (v[6] + v[7]))) + (v[8] + v[9]);
      const float mean = wave_sum(s) * (1.f / D);
      float q0 = 0.f, q1 = 0.f;
#pragma unroll
      for (int i = 0; i < 10; i += 2) {
        const float d0 = v[i] - mean, d1 = v[i + 1] - mean;
        q0 = fmaf(d0, d0, q0); q1 = fmaf(d1, d1, q1);
      }
      const float var = wave_sum(q0 + q1) * (1.f / D);
      const float m = rb(mean);
      const float inv = rcp(rb(__builtin_amdgcn_sqrtf(rb(rb(var) + 1e-5f))));
      u32 o[5];
#pragma unroll
      for (int i = 0; i < 5; ++i) {
        const u32 d = pk(v[2 * i] - m, v[2 * i + 1] - m);
        o[i] = pk(bflo(d) * inv, bfhi(d) * inv);
      }
      LDS(u32x2, S_XN + l8) = u32x2{o[0], o[1]};
      LDS(u32x2, S_XN + 512 + l8) = u32x2{o[2], o[3]};
      LDS(u32, S_XN + 1024 + l4) = o[4];
    }
    asm volatile("s_waitcnt vmcnt(0)" : "+v"(ep), "+v"(cpf) :: "memory");
    atag = __builtin_amdgcn_readfirstlane(ep) + 0x9E3779B9u;
    if (lane == 0) LDS(u32, S_TAG) = atag;
    PIN();
    if (lane == 0) LDS(u32, S_FX) = 1u;
    PIN();
    STAMP(1);
  }
  PIN();
  OPAQUE(wv3);
  if (ABL == 1) {
    RBAR();
  } else if (wv3 != 0) {
    const int q2 = c >> 2;
    u32 tw = (4 * r + q2) * 256 + ((w ^ q2) * 32) + (lane & 3) * 8;
    u32 tn = (4 * r + q2) * 128 + (((w & 3) ^ (2 * (r & 1) + (q2 >> 1))) * 32) + (lane & 3) * 8;
    u32 xad = S_XN + (80 * j + 4 * r) * 2;
    u32 pub = ((c & 1) == 0 && r == 0) ? (u32)(w * 128 + c * 8) : OOB;
    u32 fx = S_FX, fq = S_FQ;
    asm volatile("" : "+v"(tw), "+v"(tn), "+v"(xad), "+v"(pub), "+v"(fx), "+v"(fq));
    WAIT_GE(fx, 1, SLEEPS(SPIN_SL));
    STAMP(1);
    u32 tag = LDS(u32, S_TAG);
    {
      u32x2 xf[5];
#pragma unroll
      for (int n = 0; n < 5; ++n) xf[n] = LDS(u32x2, xad + n * 32);
      asm volatile("" : "+v"(xf[0]), "+v"(xf[1]), "+v"(xf[2]), "+v"(xf[3]), "+v"(xf[4]), "+v"(tag));
      WAIT_GE(pa, N_WQ, SLEEPS(SPIN2));
      STAMP(10);
      u32x2 b[5];
#pragma unroll
      for (int n = 0; n < 5; ++n) b[n] = TR_READ(tw + S_WQ + n * 4096);
      f32x4 acc = mfma(cat(xf[0], xf[1]), cat(b[0], b[1]), Z4);
      acc = mfma(cat(xf[2], xf[3]), cat(b[2], b[3]), acc);
      const u32x4 xl = cat(xf[4], u32x2{0u, 0u});
      float v = mfma(xl, cat(b[4], b[4]), acc)[0];
      PUB(v, dpp<QX1>(v), (ABL & 4) ? OOB : pub, WS_QP + lb * 1024);
      PIN();
      STAMP(2);
      if (w < 4 && ffn) {
        WAIT_GE(pa, N_W1, SLEEPS(SPIN_SL));
#pragma unroll
        for (int n = 0; n < 5; ++n) b[n] = TR_READ(tn + S_W1 + n * 2048);
        acc = mfma(cat(xf[0], xf[1]), cat(b[0], b[1]), Z4);
        acc = mfma(cat(xf[2], xf[3]), cat(b[2], b[3]), acc);
        v = mfma(xl, cat(b[4], b[4]), acc)[0];
        PUB(v, dpp<QX1>(v), (ABL & 4) ? OOB : pub, WS_HP + lb * 512);
      }
    }
    PIN();
    const bool fw = w >= 4 && j < 5;
    const int cg = (h - NFG) * 8 + j;
    const bool red_wave = w == 0 && !ffn;
    u32 qaddr[4];
#pragma unroll
    for (int i = 0; i < 4; ++i) qaddr[i] = q2 == i ? S_Q + 16 * (4 * r + (c & 3)) : S_ZERO;
    u32 kad = S_K + w * 4096 + lane * 16;
    u32 mad = S_MW + (lane & 7) * 4, mlw = S_MW + w * 4, lad = S_LW + (lane & 7) * 4;
    u32 pad_ = S_P + (32 * (w >> 1) + 8 * q2 + 4 * (w & 1) + r) * 2;
    u32 r16 = r * 16;
    u32 tv = tw + S_V;
    u32 oo = WS_OP + h * 8192 + g8 * 1024 + (16 * w + 2 * (lane & 7)) * 8;
    u32 mo = S_PW + g8 * 4;
    u32 tb = tn + (w < 4 ? S_A : S_B);
    u32 ow = S_AV + (32 * (w >> 1) + 8 * ((lane & 7) >> 1) + 4 * (w & 1) + 2 * (lane & 1) + (lane >> 5)) * 2;
    u32 ab = S_AV + (fw ? 256 : 0) + r * 16;
    float k0 = (fw && r == 1) ? 0.f : 1.f, k1 = (fw && r == 0) ? 0.f : 1.f;
    u32 so = (fw ? WS_FP + j * 1024 + r * 512 : WS_SP + (la + (w >> 2)) * 512) + h * (D * 8) + (w & 3) * 128 + c * 8;
    so = ((c & 1) == 0 && r < (fw ? 2 : 1) && (w < 4 || b_w2 || b_wo)) ? so : OOB;
    const bool ron = w < 7 || lane < 24;
    u32 ro = ron ? WS_SP + (8 * w + g8) * (D * 8) + cg * 128 + (lane & 7) * 16 : OOB;
    u32 rtag = ron ? tag : 0u;
    asm volatile("" : "+v"(qaddr[0]), "+v"(qaddr[1]), "+v"(qaddr[2]), "+v"(qaddr[3]), "+v"(kad), "+v"(mad), "+v"(mlw), "+v"(lad), "+v"(pad_),
                 "+v"(r16), "+v"(tv), "+v"(oo), "+v"(mo), "+v"(tb), "+v"(ow), "+v"(ab), "+v"(k0), "+v"(k1), "+v"(so), "+v"(ro), "+v"(rtag));
    u32 xv, bv, pre2;
    if (!ffn) {
      pre2 = PRE_EARLY ? 0u : LD32(rws, (ABL & 2) ? OOB : ro, 0, SC1);
      xv = LD32(mk(x, D * 2), 32 * cg + 4 * (lane & 7), 0, 0);
      bv = LD32(mk(b2, D * 2), 32 * cg + 4 * (lane & 7), 0, 0);
    }
    {
      WAIT_GE(pa, N_W1 + 4 * w + 4, SLEEPS(SPIN_SL));
      u32x4 kk[4];
#pragma unroll
      for (int i = 0; i < 4; ++i) kk[i] = LDS(u32x4, kad + i * 1024);
      ANCH4(kk);
      STAMP(15);
      WAIT_GE(fq, 1, SLEEPS(SPIN2));
      STAMP(3);
      u32x4 qb[4];
#pragma unroll
      for (int i = 0; i < 4; ++i) qb[i] = LDS(u32x4, qaddr[i]);
      PIN();
      f32x4 d = mfma(kk[0], qb[0], Z4);
      d = mfma(kk[1], qb[1], d); d = mfma(kk[2], qb[2], d); d = mfma(kk[3], qb[3], d);
      float sc = (c & 2) ? ((c & 1) ? d[3] : d[2]) : ((c & 1) ? d[1] : d[0]);
      sc += dpp<QX1>(sc); sc += dpp<QX2>(sc);
      sc = rb(rb(sc) * 0.08838834764831845f);
      float m = sc;
      MXDPP(m, "row_ror:4"); MXDPP(m, "row_ror:8");
      m = x32_max(x16_max(m));
      LDS(float, mlw) = m;
      float M;
      WAIT_SET(M, mad, "");
      MXDPP(M, "quad_perm:[1,0,3,2]"); MXDPP(M, "quad_perm:[2,3,0,1]"); MXDPP(M, "row_half_mirror");
      const float e = ex2((sc - M) * LOG2E);
      LDS(__bf16, pad_) = (__bf16)e;
      float l = e + dpp<ROR4>(e); l += dpp<ROR8>(l);
      l = x32_sum(x16_sum(l));
      PIN();
      LDS(float, mlw + 32) = l;
      float t;
      WAIT_SET(t, lad, "");
      STAMP(4);
      u32x4 pf[4];
#pragma unroll
      for (int n = 0; n < 4; ++n) pf[n] = LDS(u32x4, r16 + S_P + n * 64);
      WAIT_GE(pa, N_V - 8, SLEEPS(SPIN2));
      u32x2 b[8];
#pragma unroll
      for (int n = 0; n < 6; ++n) b[n] = TR_READ(tv + n * 4096);
      f32x4 acc = mfma(pf[0], cat(b[0], b[1]), Z4);
      acc = mfma(pf[1], cat(b[2], b[3]), acc);
      acc = mfma(pf[2], cat(b[4], b[5]), acc);
      PIN();
      WAIT_GE(pa, N_V, SLEEPS(SPIN2));
      STAMP(17);
      b[6] = TR_READ(tv + 6 * 4096); b[7] = TR_READ(tv + 7 * 4096);
      acc = mfma(pf[3], cat(b[6], b[7]), acc);
      if (ABL & 8) WAIT_GE(pa, N_A, SLEEPS(1));
      if (ABL & 32) __builtin_amdgcn_s_sleep(60);
      PUB(acc[0], dpp<QX1>(acc[0]), pub, WS_OP + lb * 1024);
    }
    PIN();
    STAMP(5);
    u32x2 bt[8];
    {
      if (OGAP) __builtin_amdgcn_s_sleep(OGAP);
      if (OTRIG) WAIT_GE(pa, OTRIG, SLEEPS(SPIN_SL));
      float e;
      if (PWFIRST) WAIT_SET(e, mo, "");
      PIN();
      u32x4 go = LD128(rws, oo, 0, SC1);
      if (TRFIRST) {
        if (OTRIG < N_A) WAIT_GE(pa, N_A, SLEEPS(SPIN2));
        STAMP(27);
#pragma unroll
        for (int n = 0; n < 8; ++n) bt[n] = TR_READ(tb + n * 2048);
      }
      PIN();
      POLL(go, 100, 101, 103, oo, tag, SLEEPS(SL_O));
      STAMP(25);
      if (!PWFIRST) WAIT_SET(e, mo, "");
      STAMP(26);
      auto sw = __builtin_amdgcn_permlane32_swap(asu(e * asf(go.x)), asu(e * asf(go.z)), false, false);
      float t = asf(sw[0]) + asf(sw[1]);
      t += dpp<ROR8>(t);
      t = x16_sum(t);
      LDS(__bf16, ow) = (__bf16)t;
    }
    BAR();
    STAMP(6);
    {
      u32x4 av[4];
#pragma unroll
      for (int n = 0; n < 4; ++n) {
        av[n] = LDS(u32x4, ab + n * 64);
        if (!TRFIRST) { bt[2 * n] = TR_READ(tb + 2 * n * 2048); bt[2 * n + 1] = TR_READ(tb + (2 * n + 1) * 2048); }
      }
      PIN();
      f32x4 c0 = mfma(av[0], cat(bt[0], bt[1]), Z4);
      c0 = mfma(av[1], cat(bt[2], bt[3]), c0);
      f32x4 c1 = mfma(av[2], cat(bt[4], bt[5]), Z4);
      c1 = mfma(av[3], cat(bt[6], bt[7]), c1);
      float v = k0 * c0[0];
      asm volatile("" : "+v"(v));
      v = fmaf(k1, c1[0], v);
      PUB(v, dpp<QX1>(v), so, 0);
    }
    STAMP(7);
    if (!ffn) {
      u32x4 gr = LD128(rws, ro, 0, SC1);
      u32 fw_ad = S_RED + w * 64 + (lane & 7) * 8 + (lane >> 5) * 4;
      u32 fr_ad = S_RED + g8 * 64 + (lane & 7) * 8;
      u32 oad = lane < 8 ? 32 * cg + 4 * lane : OOB;
      const rsrc_t rout = mk(out, D * 2);
      asm volatile("" : "+v"(fw_ad), "+v"(fr_ad), "+v"(oad), "+v"(xv), "+v"(bv) : "v"(pre2), "s"(rout));
      POLL(gr, 96, 97, 99, ro, rtag, SLEEPS(SL_R));
      auto sw = __builtin_amdgcn_permlane32_swap(gr.x, gr.z, false, false);
      float t = asf(sw[0]) + asf(sw[1]);
      t += dpp<ROR8>(t);
      LDS(float, fw_ad) = x16_sum(t);
      BAR();
      STAMP(8);
      if (red_wave) {
        const f32x2 u = LDS(f32x2, fr_ad);
        float u0 = u.x, u1 = u.y;
        u0 += dpp<ROR8>(u0); u1 += dpp<ROR8>(u1);
        u0 = x16_sum(u0); u1 = x16_sum(u1);
        const u32 su = pk(u0, u1);
        auto t2 = __builtin_amdgcn_permlane32_swap(su, su, false, false);
        const float y0 = rb(bflo(t2[1]) + bflo(bv)), y1 = rb(bfhi(t2[1]) + bfhi(bv));
        const float g0 = rb(y0 * rcp(1.f + ex2(-LOG2E * y0))), g1 = rb(y1 * rcp(1.f + ex2(-LOG2E * y1)));
        const u32 res = pk(bflo(xv) + rb(bflo(t2[0]) * g0), bfhi(xv) + rb(bfhi(t2[0]) * g1));
        __builtin_amdgcn_raw_buffer_store_b32(res, rout, oad, 0, 0);
      } else if (w == 1 && cg == 0) {
        __builtin_amdgcn_raw_buffer_store_b32(tag, rws, lane == 0 ? WS_E : OOB, 0, SC1);
      }
    }
  } else {
    const u32 tag = atag;
    u32 po = OOB;
    if (lane < 8) po = WS_OP + lb * 1024 + lane * 128;
    if (lane == 8) po = WS_ML + lb * 128;
    if (PRE_SF && lane >= 16 && lane < (b_wo ? 24 : 20)) po = WS_SP + h * (D * 8) + la * 512 + (lane - 16) * 128;
    if (PRE_SF && lane >= 24 && lane < 32 && b_w2) po = WS_FP + h * (D * 8) + j * 1024 + (lane - 24) * 128;
    const u32 pre = LD32(rws, (ABL & 2) ? OOB : po, 0, SC1);
    const u32 b1v = LD32(mk(b1, FF * 2), b_w2 ? (h * 64 + 2 * (lane & 31)) * 2 : OOB, 0, 0);
    u32 l16 = lane * 16;
    asm volatile("" : "+v"(l16));
    WAIT_GE(pa, QTRIG, SLEEPS(SPIN_SL));
    STAMP(11);
    {
      u32x4 g[8];
      int spins = 0;
#pragma clang loop unroll(disable)
      do {
        PIN();
#pragma unroll
        for (int p = 0; p < 8; ++p) g[p] = LD128(rws, (ABL & 16) ? OOB : l16, WS_QP + h * 8192 + p * 1024, SC1);
        PIN();
        LDS(u32, S_Q + l4) = pk(add(sx4(g), sx4(g + 4)), add(sz4(g), sz4(g + 4)));
      } while (!(ABL & 20) && !none(bad4(g, tag) | bad4(g + 4, tag)) && ++spins < SPIN_MAX);
      PIN();
      if (lane == 0) LDS(u32, S_FQ) = 1u;
      asm volatile("" :: "v"(pre));
    }
    PIN();
    STAMP(3);
    const u32 hoff = (b_w2 && !(ABL & 16)) ? (lane >> 5) * 512 + (lane & 31) * 16 : OOB;
    u32x4 gh[4];
#pragma unroll
    for (int n = 0; n < 4; ++n) gh[n] = LD128(rws, hoff, WS_HP + h * 4096 + n * 1024, SC1);
    u32 l7 = (lane & 7) * 4;
    {
      u32 mlo = lane < 8 ? WS_ML + lb * 128 + l16 : OOB, la_ = S_LW + l7;
      asm volatile("" : "+v"(mlo), "+v"(la_));
      float Ls;
      WAIT_SET(Ls, la_, "");
      float M = LDS(float, S_MW + l7);
      Ls += dpp<QX1>(Ls); Ls += dpp<QX2>(Ls); Ls += dpp<0x141>(Ls);
      MXDPP(M, "quad_perm:[1,0,3,2]"); MXDPP(M, "quad_perm:[2,3,0,1]"); MXDPP(M, "row_half_mirror");
      if (ABL & 8) WAIT_GE(pa, N_A, SLEEPS(1));
      if (ABL & 32) __builtin_amdgcn_s_sleep(60);
      PUB(M * LOG2E, Ls, mlo, 0);
    }
    PIN();
    STAMP(4);
    {
      const u32 htag = b_w2 ? tag : 0u;
      int spins = 0;
      ANCH4(gh);
#pragma clang loop unroll(disable)
      while (!(ABL & 20) && !none(bad4(gh, htag)) && ++spins < SPIN_MAX) {
        asm volatile("" ::: "memory");
#pragma unroll
        for (int n = 0; n < 4; ++n) gh[n] = LD128(rws, hoff, WS_HP + h * 4096 + n * 1024, SC1);
        ANCH4(gh);
      }
      float h0 = x32_sum(sx4(gh)), h1 = x32_sum(sz4(gh));
      h0 = rb(rb(h0) + bflo(b1v)); h1 = rb(rb(h1) + bfhi(b1v));
      const float u0 = -LOG2E * 1.5957691216057308f * (h0 + 0.044715f * h0 * h0 * h0);
      const float u1 = -LOG2E * 1.5957691216057308f * (h1 + 0.044715f * h1 * h1 * h1);
      h0 = h0 * rcp(1.f + ex2(u0)); h1 = h1 * rcp(1.f + ex2(u1));
      LDS(u32, S_AV + 256 + ((lane & 48) + 4 * ((lane >> 1) & 3) + 2 * ((lane >> 3) & 1) + (lane & 1)) * 4) = pk(h0, h1);
    }
    STAMP(14);
    {
      u32 amo = WS_ML + h * 1024 + (lane & 7) * 128, avt = tag;
      WAIT_GE(pa, N_V + MTRIG, SLEEPS(SPIN_SL));
      u32x4 gm = LD128(rws, amo, 0, SC1);
      POLL(gm, 96, 97, 99, amo, avt, SLEEPS(SL_M));
      STAMP(12);
      float M = asf(gm.x);
      MXDPP(M, "quad_perm:[1,0,3,2]"); MXDPP(M, "quad_perm:[2,3,0,1]"); MXDPP(M, "row_half_mirror");
      const float e = ex2(asf(gm.x) - M);
      float Ls = e * asf(gm.z);
      Ls += dpp<QX1>(Ls); Ls += dpp<QX2>(Ls); Ls += dpp<0x141>(Ls);
      LDS(float, S_PW + l7) = e * rcp(Ls);
      STAMP(13);
    }
    asm volatile("s_waitcnt lgkmcnt(0)" ::: "memory");
  }
#endif
}

extern "C" void kernel_launch(void* const* d_in, const int* in_sizes, int n_in, void* d_out, int out_size,
                              void* d_ws, size_t ws_size, hipStream_t stream) {
  (void)hipFuncSetAttribute((const void*)fused_decode, hipFuncAttributeMaxDynamicSharedMemorySize, LDS_BYTES);
  fused_decode<<<NWG, NTHR, LDS_BYTES, stream>>>(
      (const u16*)d_in[0], (unsigned char*)d_ws, (const u16*)d_in[3], (const u16*)d_in[1], (const u16*)d_in[6],
      (const u16*)d_in[2], (const u16*)d_in[4], (const u16*)d_in[8], (const u16*)d_in[7], (const u16*)d_in[9], (u16*)d_out);
}
```
